```python
import torch

def _softmax(x, axis=-1):
    return torch.softmax(x, dim=axis)
def _gelu(x):
    return torch.nn.functional.gelu(x, approximate="tanh")
def _silu(x):
    return torch.nn.functional.silu(x)
def _sigmoid(x):
    return torch.sigmoid(x)
def _layernorm(x, axis=-1, eps=1e-5):
    m = x.mean(dim=axis, keepdim=True)
    v = x.var(dim=axis, keepdim=True, unbiased=False)
    return (x - m) / torch.sqrt(v + eps)
def _scatter_add(tgt, idx, src, axis=0):
    return tgt.clone().index_add_(axis, idx, src)
def _segment_sum(src, seg, n):
    out = torch.zeros((n,) + src.shape[1:], dtype=src.dtype)
    return out.index_add_(0, seg, src)
def _pad(x, pw):
    flat = tuple(v for pair in reversed(pw) for v in pair)
    return torch.nn.functional.pad(x, flat)

kernel_name = 'parallel_sum__gqa_block_cached__glu_gated_swiglu__decode'

def setup_inputs():

    _w = (lambda a: (lambda t: t.to(torch.bfloat16) if t.is_floating_point() else t)(torch.as_tensor(a)))
    def _A_setup():
        B, Sq, D = 1, 1, 640
        Hq, Hkv, Sk, Dh = 32, 32, 8192, 128
        assert Hq % Hkv == 0, (Hq, Hkv)
        return {
            "x": _w((lambda *s: torch.randn(*s, dtype=torch.bfloat16) * 0.05)(B, Sq, D)),
            "k_cache": _w((lambda *s: torch.randn(*s, dtype=torch.bfloat16) * 1.6)(B, Hkv, Sk, Dh)),
            "v_cache": _w((lambda *s: torch.randn(*s, dtype=torch.bfloat16) * 0.8)(B, Hkv, Sk, Dh)),
            "wq": _w((lambda *s: torch.randn(*s, dtype=torch.bfloat16) * 1.6)(D, Hq * Dh) * (D ** -0.5)),
            "wo": _w((lambda *s: torch.randn(*s, dtype=torch.bfloat16) * 0.8)(Hq * Dh, D) * ((Hq * Dh) ** -0.5)),
        }
    _A_in = _A_setup()

    def _B_setup():
        B, S, D, F = 1, 1, 640, 1728
        return {
            "x": _w((lambda *s: torch.randn(*s, dtype=torch.bfloat16) * 0.05)(B, S, D)),
            "w1": _w((lambda *s: torch.randn(*s, dtype=torch.bfloat16) * 0.4)(D, F) * (2.0 / D) ** 0.5),
            "w3": _w((lambda *s: torch.randn(*s, dtype=torch.bfloat16) * 0.4)(D, F) * (2.0 / D) ** 0.5),
            "w2": _w((lambda *s: torch.randn(*s, dtype=torch.bfloat16) * 0.4)(F, D) * (2.0 / F) ** 0.5),
        }
    _B_in = _B_setup()

    return {
        "A_x": _A_in["x"],
        "A_k_cache": _A_in["k_cache"],
        "A_v_cache": _A_in["v_cache"],
        "A_wq": _A_in["wq"],
        "A_wo": _A_in["wo"],
        "B_x": _B_in["x"],
        "B_w1": _B_in["w1"],
        "B_w3": _B_in["w3"],
        "B_w2": _B_in["w2"],
    }

def reference(A_x, A_k_cache, A_v_cache, A_wq, A_wo, B_x, B_w1, B_w3, B_w2):
    def _A_ref(x, k_cache, v_cache, wq, wo):
        B, Sq, D = x.shape
        Hq, Dh = 32, 128
        Hkv = k_cache.shape[1]
        rep = Hq // Hkv
        q = (lambda x, *d: x.permute(*d))((x @ wq).reshape(B, Sq, Hq, Dh), 0, 2, 1, 3)
        k = (lambda x, n, axis: torch.repeat_interleave(x, n, dim=axis))(k_cache, rep, axis=1)
        v = (lambda x, n, axis: torch.repeat_interleave(x, n, dim=axis))(v_cache, rep, axis=1)
        scores = torch.einsum("bhqd,bhkd->bhqk", q, k) * (Dh ** -0.5)
        p = _softmax(scores, axis=-1)
        o = torch.einsum("bhqk,bhkd->bhqd", p, v)
        return (lambda x, *d: x.permute(*d))(o, 0, 2, 1, 3).reshape(B, Sq, Hq * Dh) @ wo

    def _B_ref(x, w1, w3, w2):
        gate = torch.einsum("bsd,df->bsf", x, w1)
        up = torch.einsum("bsd,df->bsf", x, w3)
        h = _silu(gate) * up
        out = torch.einsum("bsf,fd->bsd", h, w2)
        return out

    x = A_x
    xn = _layernorm(x, axis=-1)
    return x + _A_ref(xn, A_k_cache, A_v_cache, A_wq, A_wo) + _B_ref(xn, B_w1, B_w3, B_w2)
```

```cpp
#include <hip/hip_runtime.h>
#include <hip/hip_bf16.h>
#include <stdint.h>

typedef unsigned v4u __attribute__((ext_vector_type(4)));
using bf16x2 = __attribute__((ext_vector_type(2))) __bf16;

constexpr int D = 640, HQ = 32, DH = 128, SK = 8192, F = 1728, QD = HQ * DH;
constexpr int NSPLIT = 8, KPS = SK / NSPLIT;

__device__ __forceinline__ float bf2f(unsigned short b) { return __builtin_bit_cast(float, (unsigned)b << 16); }
__device__ __forceinline__ float bflo(unsigned u) { return __builtin_bit_cast(float, u << 16); }
__device__ __forceinline__ float bfhi(unsigned u) { return __builtin_bit_cast(float, u & 0xffff0000u); }
__device__ __forceinline__ unsigned short f2bf(float f) { unsigned u = __builtin_bit_cast(unsigned, f); u += 0x7FFFu + ((u >> 16) & 1u); return (unsigned short)(u >> 16); }
__device__ __forceinline__ float rbf(float f) { return bf2f(f2bf(f)); }
__device__ __forceinline__ float dot2(unsigned a, unsigned b, float c) {
  return __builtin_amdgcn_fdot2_f32_bf16(__builtin_bit_cast(bf16x2, a), __builtin_bit_cast(bf16x2, b), c, false);
}
__device__ __forceinline__ float wsum(float v) { for (int m = 32; m >= 1; m >>= 1) v += __shfl_xor(v, m); return v; }
__device__ __forceinline__ float wmax(float v) { for (int m = 32; m >= 1; m >>= 1) v = fmaxf(v, __shfl_xor(v, m)); return v; }

__device__ void layernorm_to_lds(const unsigned short* x, float* xn, float* red) {
  const int tid = threadIdx.x;
  float s = 0.f;
  for (int i = tid; i < D; i += blockDim.x) s += bf2f(x[i]);
  s = wsum(s);
  if ((tid & 63) == 0) red[tid >> 6] = s;
  __syncthreads();
  float tot = 0.f; for (int w = 0; w < (int)(blockDim.x >> 6); ++w) tot += red[w];
  const float mean = tot / D;
  __syncthreads();
  float v = 0.f;
  for (int i = tid; i < D; i += blockDim.x) { float d = bf2f(x[i]) - mean; v += d * d; }
  v = wsum(v);
  if ((tid & 63) == 0) red[tid >> 6] = v;
  __syncthreads();
  float tv = 0.f; for (int w = 0; w < (int)(blockDim.x >> 6); ++w) tv += red[w];
  const float rs = 1.f / sqrtf(tv / D + 1e-5f);
  for (int i = tid; i < D; i += blockDim.x) xn[i] = rbf((bf2f(x[i]) - mean) * rs);
  __syncthreads();
}

__global__ void __launch_bounds__(256) k_pre(const unsigned short* x, const unsigned short* wq, const unsigned short* w1, const unsigned short* w3,
                                             unsigned short* q, float* hbuf) {
  __shared__ float xn[D]; __shared__ float red[8]; __shared__ float part[2][4][64];
  layernorm_to_lds(x, xn, red);
  const int tid = threadIdx.x, c = tid & 63, r = tid >> 6, b = blockIdx.x;
  if (b < 64) {
    float a = 0.f;
    for (int d = r; d < D; d += 4) a += xn[d] * bf2f(wq[(size_t)d * QD + b * 64 + c]);
    part[0][r][c] = a; __syncthreads();
    if (r == 0) q[b * 64 + c] = f2bf(part[0][0][c] + part[0][1][c] + part[0][2][c] + part[0][3][c]);
  } else {
    const int f0 = (b - 64) * 64; float g = 0.f, u = 0.f;
    for (int d = r; d < D; d += 4) { g += xn[d] * bf2f(w1[(size_t)d * F + f0 + c]); u += xn[d] * bf2f(w3[(size_t)d * F + f0 + c]); }
    part[0][r][c] = g; part[1][r][c] = u; __syncthreads();
    if (r == 0) { float gg = rbf(part[0][0][c] + part[0][1][c] + part[0][2][c] + part[0][3][c]), uu = rbf(part[1][0][c] + part[1][1][c] + part[1][2][c] + part[1][3][c]);
      hbuf[f0 + c] = rbf(rbf(gg / (1.f + __expf(-gg))) * uu); }
  }
}

__global__ void __launch_bounds__(256) k_attn(const unsigned short* q, const v4u* K, const v4u* V, float* part) {
  __shared__ float sc[KPS]; __shared__ float red[8]; __shared__ float oacc[4][4][DH];
  const int h = blockIdx.x / NSPLIT, s = blockIdx.x % NSPLIT, tid = threadIdx.x, lane = tid & 63, w = tid >> 6, r = lane >> 4, c = lane & 15;
  const v4u qv = *(const v4u*)(q + h * DH + 8 * c);
  const v4u* kb = K + ((size_t)h * SK + s * KPS) * 16;
  const v4u* vb = V + ((size_t)h * SK + s * KPS) * 16;
  for (int t = w * 4; t < KPS; t += 64) {
#pragma unroll
    for (int j = 0; j < 4; ++j) {
      const int key = t + j * 16 + r;
      const v4u k = __builtin_nontemporal_load(kb + (size_t)(t + j * 16) * 16 + lane);
      float a = dot2(k.x, qv.x, 0.f); a = dot2(k.y, qv.y, a); a = dot2(k.z, qv.z, a); a = dot2(k.w, qv.w, a);
      a += __shfl_xor(a, 1); a += __shfl_xor(a, 2); a += __shfl_xor(a, 4); a += __shfl_xor(a, 8);
      if (c == 0) sc[key] = a * 0.08838834764831845f;
    }
  }
  __syncthreads();
  float m = -INFINITY; for (int i = tid; i < KPS; i += 256) m = fmaxf(m, sc[i]);
  m = wmax(m); if (lane == 0) red[w] = m; __syncthreads();
  m = fmaxf(fmaxf(red[0], red[1]), fmaxf(red[2], red[3])); __syncthreads();
  float l = 0.f; for (int i = tid; i < KPS; i += 256) { float p = __expf(sc[i] - m); sc[i] = p; l += p; }
  l = wsum(l); if (lane == 0) red[w] = l; __syncthreads();
  l = red[0] + red[1] + red[2] + red[3];
  float acc[8]; for (int k = 0; k < 8; ++k) acc[k] = 0.f;
  for (int t = w * 4; t < KPS; t += 64) {
#pragma unroll
    for (int j = 0; j < 4; ++j) {
      const int key = t + j * 16 + r;
      const v4u v = __builtin_nontemporal_load(vb + (size_t)(t + j * 16) * 16 + lane);
      const float p = sc[key];
      acc[0] += p * bflo(v.x); acc[1] += p * bfhi(v.x); acc[2] += p * bflo(v.y); acc[3] += p * bfhi(v.y);
      acc[4] += p * bflo(v.z); acc[5] += p * bfhi(v.z); acc[6] += p * bflo(v.w); acc[7] += p * bfhi(v.w);
    }
  }
  for (int k = 0; k < 8; ++k) oacc[w][r][8 * c + k] = acc[k];
  __syncthreads();
  float* po = part + (size_t)blockIdx.x * (DH + 2);
  if (tid < DH) { float o = 0.f; for (int a = 0; a < 4; ++a) for (int b = 0; b < 4; ++b) o += oacc[a][b][tid]; po[2 + tid] = o; }
  if (tid == 0) { po[0] = m; po[1] = l; }
}

__global__ void __launch_bounds__(256) k_fin(const unsigned short* x, const float* part, const float* hbuf, const unsigned short* wo, const unsigned short* w2,
                                             unsigned short* out) {
  __shared__ float o[QD]; __shared__ float red[2][16][16];
  const int tid = threadIdx.x;
  for (int i = tid; i < QD; i += 256) {
    const int h = i / DH, d = i % DH; float M = -INFINITY;
    for (int s = 0; s < NSPLIT; ++s) M = fmaxf(M, part[(size_t)(h * NSPLIT + s) * (DH + 2)]);
    float L = 0.f, a = 0.f;
    for (int s = 0; s < NSPLIT; ++s) { const float* p = part + (size_t)(h * NSPLIT + s) * (DH + 2); const float wt = __expf(p[0] - M); L += wt * p[1]; a += wt * p[2 + d]; }
    o[i] = rbf(a / L);
  }
  __syncthreads();
  const int c = tid & 15, r = tid >> 4, col = blockIdx.x * 16 + c;
  float a = 0.f, b = 0.f;
  for (int i = r; i < QD; i += 16) a += o[i] * bf2f(wo[(size_t)i * D + col]);
  for (int i = r; i < F; i += 16) b += hbuf[i] * bf2f(w2[(size_t)i * D + col]);
  red[0][r][c] = a; red[1][r][c] = b; __syncthreads();
  if (tid < 16) { float A = 0.f, B = 0.f; for (int i = 0; i < 16; ++i) { A += red[0][i][tid]; B += red[1][i][tid]; }
    const int cc = blockIdx.x * 16 + tid; out[cc] = f2bf(rbf(bf2f(x[cc]) + rbf(A)) + rbf(B)); }
}

extern "C" void kernel_launch(void* const* d_in, const int* in_sizes, int n_in, void* d_out, int out_size, void* d_ws, size_t ws_size, hipStream_t stream) {
  const unsigned short* x = (const unsigned short*)d_in[0];
  const v4u* K = (const v4u*)d_in[1]; const v4u* V = (const v4u*)d_in[2];
  const unsigned short* wq = (const unsigned short*)d_in[3]; const unsigned short* wo = (const unsigned short*)d_in[4];
  const unsigned short* w1 = (const unsigned short*)d_in[6]; const unsigned short* w3 = (const unsigned short*)d_in[7]; const unsigned short* w2 = (const unsigned short*)d_in[8];
  char* ws = (char*)d_ws;
  unsigned short* q = (unsigned short*)ws; float* hbuf = (float*)(ws + 16384); float* part = (float*)(ws + 32768);
  k_pre<<<64 + 27, 256, 0, stream>>>(x, wq, w1, w3, q, hbuf);
  k_attn<<<HQ * NSPLIT, 256, 0, stream>>>(q, K, V, part);
  k_fin<<<40, 256, 0, stream>>>(x, part, hbuf, wo, w2, (unsigned short*)d_out);
}
```

```cpp
#include <hip/hip_runtime.h>
#include <stdint.h>
#include <type_traits>

namespace {

typedef unsigned v4u __attribute__((ext_vector_type(4)));
typedef unsigned v2u __attribute__((ext_vector_type(2)));
typedef float f32x4 __attribute__((ext_vector_type(4)));
typedef float f32x2 __attribute__((ext_vector_type(2)));
typedef short bf16x8 __attribute__((ext_vector_type(8)));
typedef __bf16 bf16x2 __attribute__((ext_vector_type(2)));
#define GAS __attribute__((address_space(1)))
#define LAS __attribute__((address_space(3)))

constexpr int D = 640, DH = 128, SK = 8192, F = 1728, QD = 32 * DH;
constexpr int NSPLIT = 8, KPS = SK / NSPLIT;
constexpr int NFG = F / 64;
constexpr int NTHR = 320;

constexpr int NT_KV = 128;
constexpr int RING = 32;
constexpr int INFL = 7;
constexpr int PACE_OFF = 130;
constexpr int TAU16 = 246;
constexpr int Q_SLEEP = 32, G_SLEEP = 127, S_SLEEP = 32, O_SLEEP = 24, Y_SLEEP = 18;

constexpr unsigned WS_EPOCH = 0;
constexpr unsigned WS_T = 1024;
constexpr unsigned WS_Q = 4096;
constexpr unsigned WS_G = WS_Q + 256 * 1024;
constexpr unsigned WS_O = WS_G + 256 * 1024;
constexpr unsigned O_STRIDE = 1152;
constexpr unsigned WS_Y = WS_O + 256 * O_STRIDE;
constexpr unsigned WS_END = WS_Y + 32 * 5120;

constexpr int L_XN = 0, L_Q = 2560, L_HID = 2816, L_TAG = 3072, L_CNT = 3088, L_ST = 3104, L_REC = 3136, L_YP = L_REC + 4 * 512;
constexpr int L_END = 84 * 1024;
static_assert(L_YP + 4 * 80 * 4 <= L_END && L_REC % 16 == 0 && L_YP % 16 == 0, "LDS map");

constexpr int AUX_DEF = 0, AUX_NT = 2, AUX_SC1 = 16;
constexpr unsigned OOR = 0x80000000u;
constexpr unsigned SPIN_CAP = 1u << 15;
constexpr float LOG2E = 1.4426950408889634f;

struct Params {
  const void* x; const void* k; const void* v; const void* wq; const void* wo; const void* w1; const void* w3; const void* w2;
  void* out; void* ws;
};

template <int I, int N, class Fn> __device__ __forceinline__ void static_for(Fn&& f) {
  if constexpr (I < N) { f(std::integral_constant<int, I>{}); static_for<I + 1, N>(f); }
}
__device__ __forceinline__ float u2f(unsigned u) { return __builtin_bit_cast(float, u); }
__device__ __forceinline__ unsigned f2u(float f) { return __builtin_bit_cast(unsigned, f); }
__device__ __forceinline__ unsigned pack_bf16(float lo, float hi) { bf16x2 v = {(__bf16)lo, (__bf16)hi}; return __builtin_bit_cast(unsigned, v); }
__device__ __forceinline__ unsigned dup_bf16(float f) { return pack_bf16(f, f); }
__device__ __forceinline__ float rbf(float f) { return u2f(pack_bf16(f, f) << 16); }
__device__ __forceinline__ float bflo(unsigned u) { return u2f(u << 16); }
__device__ __forceinline__ float bfhi(unsigned u) { return u2f(u & 0xffff0000u); }
__device__ __forceinline__ float dot2(unsigned a, unsigned b, float c) {
  return __builtin_amdgcn_fdot2_f32_bf16(__builtin_bit_cast(bf16x2, a), __builtin_bit_cast(bf16x2, b), c, false);
}

constexpr int XOR1 = 0xB1, XOR2 = 0x4E, XOR7 = 0x141, ROR4 = 0x124, ROR8 = 0x128, BCAST = 0x150;
template <int CTRL> __device__ __forceinline__ unsigned dppu(unsigned x) { return (unsigned)__builtin_amdgcn_mov_dpp((int)x, CTRL, 0xf, 0xf, true); }
template <int CTRL> __device__ __forceinline__ float dpp(float x) { return u2f(dppu<CTRL>(f2u(x))); }
__device__ __forceinline__ float xrow_max(float x) {
  auto s = __builtin_amdgcn_permlane16_swap(f2u(x), f2u(x), false, false);
  x = fmaxf(u2f(s[0]), u2f(s[1]));
  auto t = __builtin_amdgcn_permlane32_swap(f2u(x), f2u(x), false, false);
  return fmaxf(u2f(t[0]), u2f(t[1]));
}
__device__ __forceinline__ float xrow_sum(float x) {
  auto s = __builtin_amdgcn_permlane16_swap(f2u(x), f2u(x), false, false);
  x = u2f(s[0]) + u2f(s[1]);
  auto t = __builtin_amdgcn_permlane32_swap(f2u(x), f2u(x), false, false);
  return u2f(t[0]) + u2f(t[1]);
}
__device__ __forceinline__ float wave_max(float x) {
  x = fmaxf(x, dpp<ROR8>(x)); x = fmaxf(x, dpp<XOR7>(x)); x = fmaxf(x, dpp<XOR2>(x)); x = fmaxf(x, dpp<XOR1>(x));
  return xrow_max(x);
}
__device__ __forceinline__ float wave_sum(float x) {
  x += dpp<ROR8>(x); x += dpp<XOR7>(x); x += dpp<XOR2>(x); x += dpp<XOR1>(x);
  return xrow_sum(x);
}
template <int BIT, int CTRL> __device__ __forceinline__ float tr_merge(float a, float b, int c) {
  const bool hi = (c & BIT) != 0;
  const float keep = hi ? b : a, give = hi ? a : b;
  return keep + dpp<CTRL>(give);
}
constexpr int bitrev4(int t) { return ((t & 1) << 3) | ((t & 2) << 1) | ((t & 4) >> 1) | ((t & 8) >> 3); }

__device__ __forceinline__ f32x4 sel_mfma(unsigned coef_dup, const v4u& mask, v4u data, f32x4 acc) {
  const v4u a = {coef_dup & mask.x, coef_dup & mask.y, coef_dup & mask.z, coef_dup & mask.w};
  return __builtin_amdgcn_mfma_f32_16x16x32_bf16(__builtin_bit_cast(bf16x8, a), __builtin_bit_cast(bf16x8, data), acc, 0, 0, 0);
}

__device__ __forceinline__ __amdgpu_buffer_rsrc_t make_rsrc(const void* p, unsigned bytes) {
  return __builtin_amdgcn_make_buffer_rsrc(const_cast<void*>(p), 0, (int)bytes, 0x00020000);
}
template <int AUX> __device__ __forceinline__ v4u bload(__amdgpu_buffer_rsrc_t rs, unsigned voff, unsigned soff) {
  return __builtin_amdgcn_raw_buffer_load_b128(rs, (int)voff, (int)soff, AUX);
}
__device__ __forceinline__ void publish(__amdgpu_buffer_rsrc_t rs, unsigned voff, unsigned soff, float a, float b, v2u tag) {
  const v4u u = {f2u(a), tag.x, f2u(b), tag.y};
  __builtin_amdgcn_raw_buffer_store_b128(u, rs, (int)voff, (int)soff, AUX_SC1);
}
__device__ __forceinline__ bool tag_ok(const v4u& u, v2u tag) { return u.y == tag.x && u.w == tag.y; }
__device__ __forceinline__ bool all_lanes(bool ok) { return __builtin_amdgcn_ballot_w64(!ok) == 0ull; }
#define WG_BARRIER() asm volatile("s_waitcnt lgkmcnt(0)\n\ts_barrier" ::: "memory")
#define LDS_WAIT() asm volatile("s_waitcnt lgkmcnt(0)" ::: "memory")
#define COMPILER_FENCE() asm volatile("" ::: "memory")

__device__ __forceinline__ unsigned clock_now() { return (unsigned)__builtin_amdgcn_s_memrealtime(); }
__device__ __forceinline__ void wait_until(unsigned t) {
  while ((int)(clock_now() - t) < 0) { }
}
constexpr unsigned due_at(int j) { return PACE_OFF + (j - 5) * TAU16 / 16; }
constexpr int done_after(int j) { return j < RING ? 0 : (2 * (j - RING + 1) < j - INFL + 1 ? 2 * (j - RING + 1) : j - INFL + 1); }

__device__ __forceinline__ void layernorm_to_lds(const unsigned (&xr)[5], LAS unsigned char* dst, int lane) {
  float v[10];
#pragma unroll
  for (int k = 0; k < 5; ++k) { v[2 * k] = bflo(xr[k]); v[2 * k + 1] = bfhi(xr[k]); }
  float s = 0.f;
#pragma unroll
  for (int k = 0; k < 10; ++k) s += v[k];
  const float mean = wave_sum(s) * (1.0f / D);
  float q = 0.f;
#pragma unroll
  for (int k = 0; k < 10; ++k) { const float d = v[k] - mean; q = fmaf(d, d, q); }
  const float var = rbf(wave_sum(q) * (1.0f / D));
  const float mb = rbf(mean);
  const float rden = 1.0f / rbf(__builtin_amdgcn_sqrtf(rbf(var + 1e-5f)));
#pragma unroll
  for (int k = 0; k < 5; ++k) {
    const float a = rbf(v[2 * k] - mb) * rden, b = rbf(v[2 * k + 1] - mb) * rden;
    *(LAS v2u*)(dst + (lane + 64 * k) * 8) = v2u{dup_bf16(a), dup_bf16(b)};
  }
}

template <bool EXTRA>
__device__ __forceinline__ void gather_sum8(__amdgpu_buffer_rsrc_t rs, unsigned soff, v2u tag, int lane, float& s0, float& s1, unsigned xoff,
                                            v4u& extra) {
  float a[8], b[8];
#pragma unroll
  for (int k = 0; k < 8; ++k) a[k] = b[k] = 0.f;
  unsigned pending = 0xffu, want = 0xffu;
  const unsigned voff = lane * 16, loff = lane < 8 ? lane * 1024 : OOR;
  for (unsigned spins = 0;; ++spins) {
    COMPILER_FENCE();
    const v4u light = bload<AUX_SC1>(rs, loff, soff);
    v4u t[8];
#pragma unroll
    for (int k = 0; k < 8; ++k) t[k] = bload<AUX_SC1>(rs, ((want >> k) & 1u) ? voff : OOR, soff + k * 1024);
    bool xok = true;
    if constexpr (EXTRA) { extra = bload<AUX_SC1>(rs, xoff, 0); xok = all_lanes(lane >= 8 || tag_ok(extra, tag)); }
    const unsigned seen = (unsigned)__builtin_amdgcn_ballot_w64(tag_ok(light, tag)) & 0xffu;
    unsigned got = 0;
#pragma unroll
    for (int k = 0; k < 8; ++k) got |= (all_lanes(tag_ok(t[k], tag)) ? 1u : 0u) << k;
    got &= want;
#pragma unroll
    for (int k = 0; k < 8; ++k) {
      const bool gk = (got >> k) & 1u;
      a[k] = gk ? u2f(t[k].x) : a[k];
      b[k] = gk ? u2f(t[k].z) : b[k];
    }
    pending &= ~got;
    if ((pending == 0u && xok) || spins > SPIN_CAP) break;
    want = __builtin_popcount(pending) <= 2 ? pending : (pending & seen);
    __builtin_amdgcn_s_sleep(1);
  }
  s0 = 0.f; s1 = 0.f;
#pragma unroll
  for (int k = 0; k < 8; ++k) { s0 += a[k]; s1 += b[k]; }
}

struct Ctx {
  LAS unsigned char* L;
  int lane, g, c, b, h, s, wave;
  unsigned t_start;
  v4u mask;
};

__device__ __forceinline__ void stream_role(const Params& p, const Ctx& x) {
  LAS unsigned char* L = x.L;
  const int lane = x.lane, c = x.c, g = x.g, h = x.h, s = x.s, w = x.wave;
  const v4u mask = x.mask;
  const bool has_ffn = h < NFG;
  const size_t slice = ((size_t)h * SK + (size_t)s * KPS) * DH * 2 + w * 1024;
  const __amdgpu_buffer_rsrc_t rsK = make_rsrc((const char*)p.k + slice, KPS * DH * 2 - w * 1024);
  const __amdgpu_buffer_rsrc_t rsV = make_rsrc((const char*)p.v + slice, KPS * DH * 2 - w * 1024);
  const __amdgpu_buffer_rsrc_t rsO = make_rsrc((const char*)p.wo + ((size_t)(h * DH + 32 * w) * D + 80 * s) * 2, 32 * D * 2 - 160 * s);
  const __amdgpu_buffer_rsrc_t rs2 =
      make_rsrc((const char*)p.w2 + (has_ffn ? ((size_t)(h * 64 + 16 * w) * D + 80 * s) * 2 : 0), has_ffn ? 16 * D * 2 - 160 * s : 0);
  const unsigned voff = lane * 16, vo = c < 10 ? g * (D * 2) + c * 16 : OOR;
  auto ld = [&](auto jc) -> v4u {
    constexpr int j = decltype(jc)::value;
    if constexpr (j < 64) return bload<AUX_NT>(rsK, voff, j * 4096);
    else return bload<AUX_NT>(rsV, voff, (j - 64) * 4096);
  };
  auto ldw = [&](auto tc) -> v4u {
    constexpr int t = decltype(tc)::value;
    if constexpr (t < 8) return bload<AUX_DEF>(rsO, vo, t * 4 * D * 2);
    else return bload<AUX_DEF>(rs2, vo, (t - 8) * 4 * D * 2);
  };
  v4u ring[RING], wt[12];
  unsigned tz = x.t_start;

  asm volatile("s_barrier" ::: "memory");
  static_for<0, RING>([&](auto jc) {
    constexpr int j = decltype(jc)::value;
    if constexpr (j >= INFL) asm volatile("s_waitcnt vmcnt(%0)" ::"i"(INFL - 1) : "memory");
    if constexpr (j >= 5) wait_until(tz + due_at(j));
    ring[j] = ld(jc);
    __builtin_amdgcn_sched_barrier(0);
  });
  WG_BARRIER();
  const v4u q = *(const LAS v4u*)(L + L_Q + c * 16);
  const v4u tg = *(const LAS v4u*)(L + L_TAG);
  const v2u tag = {tg.x, tg.y};
  tz = (unsigned)__builtin_amdgcn_readfirstlane((int)tg.z);

  float st0 = 0.f, st1 = 0.f, st2 = 0.f, st3 = 0.f;
  float sc[4];
  unsigned pd[4];
  f32x4 acc = {0.f, 0.f, 0.f, 0.f};
  auto process = [&](auto tc) {
    constexpr int T = decltype(tc)::value;
    const v4u d = ring[T % RING];
    if constexpr (T < 64) {
      constexpr int t = T & 15;
      float v = dot2(d.x, q.x, 0.f); v = dot2(d.y, q.y, v); v = dot2(d.z, q.z, v); v = dot2(d.w, q.w, v);
      if constexpr ((t & 1) == 0) st0 = v;
      else {
        v = tr_merge<8, ROR8>(st0, v, c);
        if constexpr ((t & 2) == 0) st1 = v;
        else {
          v = tr_merge<4, XOR7>(st1, v, c);
          if constexpr ((t & 4) == 0) st2 = v;
          else {
            v = tr_merge<2, XOR2>(st2, v, c);
            if constexpr ((t & 8) == 0) st3 = v;
            else sc[T >> 4] = tr_merge<1, XOR1>(st3, v, c);
          }
        }
      }
      if constexpr (T == 63) {
        float sv[4];
#pragma unroll
        for (int i = 0; i < 4; ++i) sv[i] = rbf(rbf(sc[i]) * 0.08838834764831845f);
        const float mw = wave_max(fmaxf(fmaxf(sv[0], sv[1]), fmaxf(sv[2], sv[3])));
        float e = 0.f;
#pragma unroll
        for (int i = 0; i < 4; ++i) { const float pe = __builtin_amdgcn_exp2f((sv[i] - mw) * LOG2E); e += pe; pd[i] = dup_bf16(pe); }
        const float lw = wave_sum(e);
        if (lane == 0) {
          *(LAS f32x2*)(L + L_ST + w * 8) = f32x2{mw, lw};
          __hip_atomic_fetch_add((LAS unsigned*)(L + L_CNT), 1u, __ATOMIC_RELAXED, __HIP_MEMORY_SCOPE_WORKGROUP);
        }
      }
    } else {
      constexpr int t = (T - 64) & 15, G = (T - 64) >> 4;
      acc = sel_mfma(dppu<BCAST + bitrev4(t)>(pd[G]), mask, d, acc);
    }
  };
  static_for<RING, NT_KV>([&](auto jc) {
    constexpr int j = decltype(jc)::value;
    asm volatile("s_waitcnt vmcnt(%0)" ::"i"(INFL - 1) : "memory");
    wait_until(tz + due_at(j));
    static_for<done_after(j - 1), done_after(j)>([&](auto tc) { process(tc); });
    ring[j % RING] = ld(jc);
    __builtin_amdgcn_sched_barrier(0);
  });
  static_for<done_after(NT_KV - 1), NT_KV>([&](auto tc) {
    constexpr int i = decltype(tc)::value - done_after(NT_KV - 1), n = NT_KV - done_after(NT_KV - 1);
    process(tc);
    static_for<12 * i / n, 12 * (i + 1) / n>([&](auto wc) {
      constexpr int k = (decltype(wc)::value + 8) % 12;
      wt[k] = ldw(std::integral_constant<int, k>{});
    });
    __builtin_amdgcn_sched_barrier(0);
  });
  if (lane < 32) *(LAS f32x4*)(L + L_REC + w * 512 + (8 * c + 4 * g) * 4) = acc;
  WG_BARRIER();

  const __amdgpu_buffer_rsrc_t rsW = make_rsrc(p.ws, WS_END);
  unsigned cf[4];
#pragma unroll
  for (int t = 0; t < 4; ++t) cf[t] = *(const LAS unsigned*)(L + L_HID + (16 * w + 4 * t + g) * 4);
  __builtin_amdgcn_s_sleep(O_SLEEP);
  v4u ga, gb;
  {
    const unsigned go = g * O_STRIDE + c * 16, so = WS_O + h * NSPLIT * O_STRIDE + 256 * w;
    auto fetch = [&]() {
      COMPILER_FENCE();
      ga = bload<AUX_SC1>(rsW, go, so);
      gb = bload<AUX_SC1>(rsW, go, so + 4 * O_STRIDE);
    };
    auto arrived = [&]() { return all_lanes(tag_ok(ga, tag) && tag_ok(gb, tag)); };
    fetch();
    if (__builtin_expect(!arrived(), 0)) {
      for (unsigned spins = 0; spins < SPIN_CAP; ++spins) {
        fetch();
        if (arrived()) break;
      }
    }
  }
  {
    const float o0 = xrow_sum(u2f(ga.x) + u2f(gb.x)), o1 = xrow_sum(u2f(ga.z) + u2f(gb.z));
    const int pick = (int)dup_bf16((g & 1) ? o1 : o0);
    const unsigned od = (unsigned)__builtin_amdgcn_update_dpp(pick, pick, ROR8, 0xc, 0xf, false);
    f32x4 acc0 = {0.f, 0.f, 0.f, 0.f}, acc1 = {0.f, 0.f, 0.f, 0.f};
#pragma unroll
    for (int t = 0; t < 4; ++t) acc1 = sel_mfma(cf[t], mask, wt[8 + t], acc1);
    static_for<0, 8>([&](auto tc) {
      constexpr int t = decltype(tc)::value;
      if constexpr (t & 1) acc1 = sel_mfma(dppu<BCAST + t>(od), mask, wt[t], acc1);
      else acc0 = sel_mfma(dppu<BCAST + t>(od), mask, wt[t], acc0);
    });
    acc0 += acc1;
    if (g < 2 && c < 10) *(LAS f32x4*)(L + L_YP + (w * 80 + 8 * c + 4 * g) * 4) = acc0;
  }
  WG_BARRIER();
  if (w == 0) {
    const int l2 = lane < 40 ? lane : 0;
    float y0 = 0.f, y1 = 0.f;
#pragma unroll
    for (int w4 = 0; w4 < 4; ++w4) { const f32x2 t = *(const LAS f32x2*)(L + L_YP + (w4 * 80 + 2 * l2) * 4); y0 += t.x; y1 += t.y; }
    publish(rsW, lane < 40 ? lane * 16 : OOR, WS_Y + h * 5120 + s * 640, y0, y1, tag);
  }
}

__device__ __forceinline__ void side_role(const Params& p, const Ctx& x) {
  LAS unsigned char* L = x.L;
  const int lane = x.lane, c = x.c, g = x.g, h = x.h, s = x.s, b = x.b;
  const v4u mask = x.mask;
  const __amdgpu_buffer_rsrc_t rsW = make_rsrc(p.ws, WS_END);
  const unsigned rec_id = h * NSPLIT + s;
  GAS unsigned long long* epoch = (GAS unsigned long long*)((char*)p.ws + WS_EPOCH);

  __builtin_amdgcn_s_setprio(3);
  const unsigned long long e = __hip_atomic_load(epoch, __ATOMIC_RELAXED, __HIP_MEMORY_SCOPE_AGENT);
  unsigned xr[5];
#pragma unroll
  for (int k = 0; k < 5; ++k) xr[k] = ((const GAS unsigned*)p.x)[lane + 64 * k];
  v4u w[20];
  {
    const __amdgpu_buffer_rsrc_t rs = make_rsrc(p.wq, D * QD * 2);
#pragma unroll
    for (int t = 0; t < 20; ++t) w[t] = bload<AUX_DEF>(rs, g * (QD * 2) + c * 16, ((80 * s + 4 * t) * QD + h * DH) * 2);
  }
  const unsigned xres = ((const GAS unsigned*)p.x)[(b < 160 ? 2 * b : 0) + (lane & 1)];
  __builtin_amdgcn_sched_barrier(0);
  asm volatile("s_barrier" ::: "memory");
  const v2u tag = {(unsigned)e + 1u, (unsigned)(e >> 32) + 1u};
  publish(rsW, (b < 8 && lane == 0) ? WS_T + b * 128 : OOR, 0, u2f(x.t_start), 0.f, tag);
  layernorm_to_lds(xr, L + L_XN, lane);
  LDS_WAIT();
  {
    unsigned cf[20];
#pragma unroll
    for (int t = 0; t < 20; ++t) cf[t] = *(const LAS unsigned*)(L + L_XN + (80 * s + 4 * t + g) * 4);
    f32x4 acc0 = {0.f, 0.f, 0.f, 0.f}, acc1 = {0.f, 0.f, 0.f, 0.f};
#pragma unroll
    for (int t = 0; t < 20; t += 2) { acc0 = sel_mfma(cf[t], mask, w[t], acc0); acc1 = sel_mfma(cf[t + 1], mask, w[t + 1], acc1); }
    acc0 += acc1;
    if (lane < 32) {
      const unsigned vo = (4 * c + 2 * g) * 16;
      publish(rsW, vo, WS_Q + rec_id * 1024, acc0[0], acc0[1], tag);
      publish(rsW, vo + 16, WS_Q + rec_id * 1024, acc0[2], acc0[3], tag);
    }
    __builtin_amdgcn_s_sleep(Q_SLEEP);
    float q0, q1;
    v4u tu;
    gather_sum8<true>(rsW, WS_Q + h * NSPLIT * 1024, tag, lane, q0, q1, lane < 8 ? WS_T + lane * 128 : OOR, tu);
    int dt = lane < 8 ? (int)(tu.x - x.t_start) : 0;
    dt = min(dt, (int)dppu<XOR1>((unsigned)dt)); dt = min(dt, (int)dppu<XOR2>((unsigned)dt)); dt = min(dt, (int)dppu<XOR7>((unsigned)dt));
    dt = max(min(dt, 0), -200);
    *(LAS unsigned*)(L + L_Q + lane * 4) = pack_bf16(q0, q1);
    if (lane == 0) { *(LAS v4u*)(L + L_TAG) = v4u{tag.x, tag.y, x.t_start + (unsigned)dt, 0u}; *(LAS unsigned*)(L + L_CNT) = 0u; }
  }
  WG_BARRIER();
  __builtin_amdgcn_s_setprio(0);
  if (h < NFG) {
    const __amdgpu_buffer_rsrc_t rs1 = make_rsrc(p.w1, D * F * 2), rs3 = make_rsrc(p.w3, D * F * 2);
    const unsigned vo = (4 * (c >> 3) + g) * (F * 2) + (c & 7) * 16;
#pragma unroll
    for (int t = 0; t < 10; ++t) {
      w[2 * t] = bload<AUX_DEF>(rs1, vo, ((80 * s + 8 * t) * F + h * 64) * 2);
      w[2 * t + 1] = bload<AUX_DEF>(rs3, vo, ((80 * s + 8 * t) * F + h * 64) * 2);
    }
    unsigned cf[10];
#pragma unroll
    for (int t = 0; t < 10; ++t) cf[t] = *(const LAS unsigned*)(L + L_XN + (80 * s + 8 * t + 4 * (c >> 3) + g) * 4);
    f32x4 ag = {0.f, 0.f, 0.f, 0.f}, au = {0.f, 0.f, 0.f, 0.f};
#pragma unroll
    for (int t = 0; t < 10; ++t) { ag = sel_mfma(cf[t], mask, w[2 * t], ag); au = sel_mfma(cf[t], mask, w[2 * t + 1], au); }
#pragma unroll
    for (int r = 0; r < 4; ++r) { ag[r] += __shfl(ag[r], (lane + 40) & 63); au[r] += __shfl(au[r], (lane + 40) & 63); }
    if (g < 2 && c < 8) {
      const unsigned uo = (4 * c + 2 * g) * 16;
      publish(rsW, uo, WS_G + rec_id * 1024, ag[0], ag[1], tag);
      publish(rsW, uo + 16, WS_G + rec_id * 1024, ag[2], ag[3], tag);
      publish(rsW, uo + 512, WS_G + rec_id * 1024, au[0], au[1], tag);
      publish(rsW, uo + 528, WS_G + rec_id * 1024, au[2], au[3], tag);
    }
    __builtin_amdgcn_s_sleep(G_SLEEP);
    float a0, a1;
    v4u none;
    gather_sum8<false>(rsW, WS_G + h * NSPLIT * 1024, tag, lane, a0, a1, 0u, none);
    a0 = rbf(a0); a1 = rbf(a1);
    const float u0 = __shfl(a0, (lane + 32) & 63), u1 = __shfl(a1, (lane + 32) & 63);
    const float h0 = rbf(a0 / (1.0f + __expf(-a0))) * u0, h1 = rbf(a1 / (1.0f + __expf(-a1))) * u1;
    if (lane < 32) *(LAS v2u*)(L + L_HID + lane * 8) = v2u{dup_bf16(h0), dup_bf16(h1)};
  } else {
    if (lane < 32) *(LAS v2u*)(L + L_HID + lane * 8) = v2u{0u, 0u};
  }
  float fac[4];
  {
    for (unsigned spins = 0; spins < (SPIN_CAP << 4); ++spins) {
      if (__hip_atomic_load((LAS unsigned*)(L + L_CNT), __ATOMIC_RELAXED, __HIP_MEMORY_SCOPE_WORKGROUP) >= 4u) break;
      __builtin_amdgcn_s_sleep(4);
    }
    COMPILER_FENCE();
    float m[4], l[4];
#pragma unroll
    for (int w4 = 0; w4 < 4; ++w4) { const f32x2 t = *(const LAS f32x2*)(L + L_ST + w4 * 8); m[w4] = t.x; l[w4] = t.y; }
    const float M = fmaxf(fmaxf(m[0], m[1]), fmaxf(m[2], m[3]));
    float ls = 0.f;
#pragma unroll
    for (int w4 = 0; w4 < 4; ++w4) ls = fmaf(__builtin_amdgcn_exp2f((m[w4] - M) * LOG2E), l[w4], ls);
    publish(rsW, lane == 0 ? 1024u : OOR, WS_O + rec_id * O_STRIDE, M, ls, tag);
    __builtin_amdgcn_s_sleep(S_SLEEP);
    v4u ml;
    for (unsigned spins = 0; spins < SPIN_CAP; ++spins) {
      COMPILER_FENCE();
      ml = bload<AUX_SC1>(rsW, lane < 8 ? 1024 + lane * O_STRIDE : OOR, WS_O + h * NSPLIT * O_STRIDE);
      if (all_lanes(lane >= 8 || tag_ok(ml, tag))) break;
      __builtin_amdgcn_s_sleep(16);
    }
    const float mk = u2f(ml.x);
    float MM = fmaxf(mk, dpp<XOR1>(mk)); MM = fmaxf(MM, dpp<XOR2>(MM)); MM = fmaxf(MM, dpp<XOR7>(MM));
    float LL = __builtin_amdgcn_exp2f((mk - MM) * LOG2E) * u2f(ml.z);
    LL += dpp<XOR1>(LL); LL += dpp<XOR2>(LL); LL += dpp<XOR7>(LL);
    const float Mh = u2f((unsigned)__builtin_amdgcn_readfirstlane((int)f2u(MM)));
    const float rl = 1.0f / u2f((unsigned)__builtin_amdgcn_readfirstlane((int)f2u(LL)));
#pragma unroll
    for (int w4 = 0; w4 < 4; ++w4) fac[w4] = __builtin_amdgcn_exp2f((m[w4] - Mh) * LOG2E) * rl;
  }
  __builtin_amdgcn_s_setprio(3);
  WG_BARRIER();
  {
    float o0 = 0.f, o1 = 0.f;
#pragma unroll
    for (int w4 = 0; w4 < 4; ++w4) {
      const f32x2 t = *(const LAS f32x2*)(L + L_REC + w4 * 512 + (32 * g + 4 * (c & 7) + 2 * (c >> 3)) * 4);
      o0 = fmaf(fac[w4], t.x, o0); o1 = fmaf(fac[w4], t.y, o1);
    }
    publish(rsW, lane * 16, WS_O + rec_id * O_STRIDE, o0, o1, tag);
  }
  WG_BARRIER();
  if (b < 160) {
    __builtin_amdgcn_s_sleep(Y_SLEEP);
    const unsigned yo = (lane >> 1) * 5120 + (lane & 1) * 16, ys = WS_Y + b * 32;
    v4u u, ua = bload<AUX_SC1>(rsW, yo, ys);
    for (unsigned spins = 0;; ++spins) {
      __builtin_amdgcn_s_sleep(4);
      COMPILER_FENCE();
      const v4u ub = bload<AUX_SC1>(rsW, yo, ys);
      u = ua;
      if (all_lanes(tag_ok(ua, tag))) break;
      __builtin_amdgcn_s_sleep(4);
      COMPILER_FENCE();
      ua = bload<AUX_SC1>(rsW, yo, ys);
      u = ub;
      if (all_lanes(tag_ok(ub, tag)) || spins > SPIN_CAP) break;
    }
    float y0 = u2f(u.x), y1 = u2f(u.z);
    y0 += dpp<XOR2>(y0); y1 += dpp<XOR2>(y1);
    y0 += dpp<ROR4>(y0); y1 += dpp<ROR4>(y1);
    y0 += dpp<ROR8>(y0); y1 += dpp<ROR8>(y1);
    y0 = xrow_sum(y0); y1 = xrow_sum(y1);
    if (lane < 2) ((GAS unsigned*)p.out)[2 * b + lane] = pack_bf16(bflo(xres) + y0, bfhi(xres) + y1);
    if (b == 0 && lane == 0) __hip_atomic_store(epoch, ((unsigned long long)tag.y << 32) | tag.x, __ATOMIC_RELAXED, __HIP_MEMORY_SCOPE_AGENT);
  }
}

__global__ void __launch_bounds__(NTHR, 2) fused_decode_kernel(Params p) {
  __shared__ __attribute__((aligned(16))) unsigned char lds_raw[L_END];
  Ctx x;
  x.L = (LAS unsigned char*)lds_raw;
  const int tid = threadIdx.x;
  x.lane = tid & 63; x.wave = __builtin_amdgcn_readfirstlane(tid >> 6);
  x.b = blockIdx.x; x.h = (x.b & 7) * 4 + (x.b >> 6); x.s = (x.b >> 3) & 7;
  x.g = x.lane >> 4; x.c = x.lane & 15;
  {
    const int j = x.c & 7;
    const unsigned half = (j & 1) ? 0xffff0000u : 0x0000ffffu;
    x.mask = v4u{(j >> 1) == 0 ? half : 0u, (j >> 1) == 1 ? half : 0u, (j >> 1) == 2 ? half : 0u, (j >> 1) == 3 ? half : 0u};
  }
  asm volatile("" ::"s"(p.x), "s"(p.k), "s"(p.v), "s"(p.wq), "s"(p.wo), "s"(p.w1), "s"(p.w3), "s"(p.w2), "s"(p.out), "s"(p.ws));
  x.t_start = clock_now();
  if (x.wave < 4) stream_role(p, x);
  else side_role(p, x);
}

}

extern "C" void kernel_launch(void* const* d_in, const int* in_sizes, int n_in, void* d_out, int out_size, void* d_ws, size_t ws_size,
                              hipStream_t stream) {
  Params p;
  p.x = d_in[0]; p.k = d_in[1]; p.v = d_in[2]; p.wq = d_in[3]; p.wo = d_in[4];
  p.w1 = d_in[6]; p.w3 = d_in[7]; p.w2 = d_in[8];
  p.out = d_out; p.ws = d_ws;
  hipLaunchKernelGGL(fused_decode_kernel, dim3(256), dim3(NTHR), 0, stream, p);
}
```

```cpp
#include <hip/hip_runtime.h>
#include <stdint.h>
#include <type_traits>

namespace {

typedef unsigned v4u __attribute__((ext_vector_type(4)));
typedef unsigned v2u __attribute__((ext_vector_type(2)));
typedef float f32x4 __attribute__((ext_vector_type(4)));
typedef float f32x2 __attribute__((ext_vector_type(2)));
typedef short bf16x8 __attribute__((ext_vector_type(8)));
typedef __bf16 bf16x2 __attribute__((ext_vector_type(2)));
#define GAS __attribute__((address_space(1)))
#define LAS __attribute__((address_space(3)))

constexpr int D = 640, DH = 128, SK = 8192, F = 1728, QD = 32 * DH;
constexpr int NSPLIT = 8, KPS = SK / NSPLIT;
constexpr int NFG = F / 64;
constexpr int NTHR = 320;

constexpr int NT_KV = 128;
constexpr int RING = 40;
constexpr int INFL = 7;
constexpr int PACE_OFF = 130;
constexpr int TAU16 = 246;
constexpr int PRE_LEAD = 150;
constexpr int Q_SLEEP = 32, G_SLEEP = 127, S_SLEEP = 32, O_SLEEP = 24, Y_SLEEP = 18;

constexpr unsigned WS_EPOCH = 0;
constexpr unsigned WS_T = 1024;
constexpr unsigned WS_Q = 4096;
constexpr unsigned WS_G = WS_Q + 256 * 1024;
constexpr unsigned WS_O = WS_G + 256 * 1024;
constexpr unsigned O_STRIDE = 1152;
constexpr unsigned WS_Y = WS_O + 256 * O_STRIDE;
constexpr unsigned WS_END = WS_Y + 32 * 5120;

constexpr int L_XN = 0, L_Q = 2560, L_HID = 2816, L_TAG = 3072, L_CNT = 3088, L_ST = 3104, L_REC = 3136, L_YP = L_REC + 4 * 512;
constexpr int L_END = 84 * 1024;
static_assert(L_YP + 4 * 80 * 4 <= L_END && L_REC % 16 == 0 && L_YP % 16 == 0, "LDS map");

constexpr int AUX_DEF = 0, AUX_NT = 2, AUX_SC1 = 16;
constexpr unsigned OOR = 0x80000000u;
constexpr unsigned SPIN_CAP = 1u << 15;
constexpr float LOG2E = 1.4426950408889634f;

struct Params {
  const void* x; const void* k; const void* v; const void* wq; const void* wo; const void* w1; const void* w3; const void* w2;
  void* out; void* ws;
};

template <int I, int N, class Fn> __device__ __forceinline__ void static_for(Fn&& f) {
  if constexpr (I < N) { f(std::integral_constant<int, I>{}); static_for<I + 1, N>(f); }
}
__device__ __forceinline__ float u2f(unsigned u) { return __builtin_bit_cast(float, u); }
__device__ __forceinline__ unsigned f2u(float f) { return __builtin_bit_cast(unsigned, f); }
__device__ __forceinline__ unsigned pack_bf16(float lo, float hi) { bf16x2 v = {(__bf16)lo, (__bf16)hi}; return __builtin_bit_cast(unsigned, v); }
__device__ __forceinline__ unsigned dup_bf16(float f) { return pack_bf16(f, f); }
__device__ __forceinline__ float rbf(float f) { return u2f(pack_bf16(f, f) << 16); }
__device__ __forceinline__ float bflo(unsigned u) { return u2f(u << 16); }
__device__ __forceinline__ float bfhi(unsigned u) { return u2f(u & 0xffff0000u); }
__device__ __forceinline__ float dot2(unsigned a, unsigned b, float c) {
  return __builtin_amdgcn_fdot2_f32_bf16(__builtin_bit_cast(bf16x2, a), __builtin_bit_cast(bf16x2, b), c, false);
}

constexpr int XOR1 = 0xB1, XOR2 = 0x4E, XOR7 = 0x141, ROR4 = 0x124, ROR8 = 0x128, BCAST = 0x150;
template <int CTRL> __device__ __forceinline__ unsigned dppu(unsigned x) { return (unsigned)__builtin_amdgcn_mov_dpp((int)x, CTRL, 0xf, 0xf, true); }
template <int CTRL> __device__ __forceinline__ float dpp(float x) { return u2f(dppu<CTRL>(f2u(x))); }
__device__ __forceinline__ float xrow_max(float x) {
  auto s = __builtin_amdgcn_permlane16_swap(f2u(x), f2u(x), false, false);
  x = fmaxf(u2f(s[0]), u2f(s[1]));
  auto t = __builtin_amdgcn_permlane32_swap(f2u(x), f2u(x), false, false);
  return fmaxf(u2f(t[0]), u2f(t[1]));
}
__device__ __forceinline__ float xrow_sum(float x) {
  auto s = __builtin_amdgcn_permlane16_swap(f2u(x), f2u(x), false, false);
  x = u2f(s[0]) + u2f(s[1]);
  auto t = __builtin_amdgcn_permlane32_swap(f2u(x), f2u(x), false, false);
  return u2f(t[0]) + u2f(t[1]);
}
__device__ __forceinline__ float wave_max(float x) {
  x = fmaxf(x, dpp<ROR8>(x)); x = fmaxf(x, dpp<XOR7>(x)); x = fmaxf(x, dpp<XOR2>(x)); x = fmaxf(x, dpp<XOR1>(x));
  return xrow_max(x);
}
__device__ __forceinline__ float wave_sum(float x) {
  x += dpp<ROR8>(x); x += dpp<XOR7>(x); x += dpp<XOR2>(x); x += dpp<XOR1>(x);
  return xrow_sum(x);
}
template <int BIT, int CTRL> __device__ __forceinline__ float tr_merge(float a, float b, int c) {
  const bool hi = (c & BIT) != 0;
  const float keep = hi ? b : a, give = hi ? a : b;
  return keep + dpp<CTRL>(give);
}
constexpr int bitrev4(int t) { return ((t & 1) << 3) | ((t & 2) << 1) | ((t & 4) >> 1) | ((t & 8) >> 3); }

__device__ __forceinline__ f32x4 sel_mfma(unsigned coef_dup, const v4u& mask, v4u data, f32x4 acc) {
  const v4u a = {coef_dup & mask.x, coef_dup & mask.y, coef_dup & mask.z, coef_dup & mask.w};
  return __builtin_amdgcn_mfma_f32_16x16x32_bf16(__builtin_bit_cast(bf16x8, a), __builtin_bit_cast(bf16x8, data), acc, 0, 0, 0);
}

__device__ __forceinline__ __amdgpu_buffer_rsrc_t make_rsrc(const void* p, unsigned bytes) {
  return __builtin_amdgcn_make_buffer_rsrc(const_cast<void*>(p), 0, (int)bytes, 0x00020000);
}
template <int AUX> __device__ __forceinline__ v4u bload(__amdgpu_buffer_rsrc_t rs, unsigned voff, unsigned soff) {
  return __builtin_amdgcn_raw_buffer_load_b128(rs, (int)voff, (int)soff, AUX);
}
__device__ __forceinline__ void publish(__amdgpu_buffer_rsrc_t rs, unsigned voff, unsigned soff, float a, float b, v2u tag) {
  const v4u u = {f2u(a), tag.x, f2u(b), tag.y};
  __builtin_amdgcn_raw_buffer_store_b128(u, rs, (int)voff, (int)soff, AUX_SC1);
}
__device__ __forceinline__ bool tag_ok(const v4u& u, v2u tag) { return u.y == tag.x && u.w == tag.y; }
__device__ __forceinline__ bool all_lanes(bool ok) { return __builtin_amdgcn_ballot_w64(!ok) == 0ull; }
#define WG_BARRIER() asm volatile("s_waitcnt lgkmcnt(0)\n\ts_barrier" ::: "memory")
#define LDS_WAIT() asm volatile("s_waitcnt lgkmcnt(0)" ::: "memory")
#define COMPILER_FENCE() asm volatile("" ::: "memory")

__device__ __forceinline__ unsigned clock_now() { return (unsigned)__builtin_amdgcn_s_memrealtime(); }
__device__ __forceinline__ void wait_until(unsigned t) {
  while ((int)(clock_now() - t) < 0) { }
}
constexpr unsigned due_at(int j) { return PACE_OFF + (j - 5) * TAU16 / 16; }
constexpr int done_after(int j) { return j < RING ? 0 : (2 * (j - RING + 1) < j - INFL + 1 ? 2 * (j - RING + 1) : j - INFL + 1); }

__device__ __forceinline__ void layernorm_to_lds(const unsigned (&xr)[5], LAS unsigned char* dst, int lane) {
  float v[10];
#pragma unroll
  for (int k = 0; k < 5; ++k) { v[2 * k] = bflo(xr[k]); v[2 * k + 1] = bfhi(xr[k]); }
  float s = 0.f;
#pragma unroll
  for (int k = 0; k < 10; ++k) s += v[k];
  const float mean = wave_sum(s) * (1.0f / D);
  float q = 0.f;
#pragma unroll
  for (int k = 0; k < 10; ++k) { const float d = v[k] - mean; q = fmaf(d, d, q); }
  const float var = rbf(wave_sum(q) * (1.0f / D));
  const float mb = rbf(mean);
  const float rden = 1.0f / rbf(__builtin_amdgcn_sqrtf(rbf(var + 1e-5f)));
#pragma unroll
  for (int k = 0; k < 5; ++k) {
    const float a = rbf(v[2 * k] - mb) * rden, b = rbf(v[2 * k + 1] - mb) * rden;
    *(LAS v2u*)(dst + (lane + 64 * k) * 8) = v2u{dup_bf16(a), dup_bf16(b)};
  }
}

template <bool EXTRA>
__device__ __forceinline__ void gather_sum8(__amdgpu_buffer_rsrc_t rs, unsigned soff, v2u tag, int lane, float& s0, float& s1, unsigned xoff,
                                            v4u& extra) {
  float a[8], b[8];
#pragma unroll
  for (int k = 0; k < 8; ++k) a[k] = b[k] = 0.f;
  unsigned pending = 0xffu, want = 0xffu;
  const unsigned voff = lane * 16, loff = lane < 8 ? lane * 1024 : OOR;
  for (unsigned spins = 0;; ++spins) {
    COMPILER_FENCE();
    const v4u light = bload<AUX_SC1>(rs, loff, soff);
    v4u t[8];
#pragma unroll
    for (int k = 0; k < 8; ++k) t[k] = bload<AUX_SC1>(rs, ((want >> k) & 1u) ? voff : OOR, soff + k * 1024);
    bool xok = true;
    if constexpr (EXTRA) { extra = bload<AUX_SC1>(rs, xoff, 0); xok = all_lanes(lane >= 8 || tag_ok(extra, tag)); }
    const unsigned seen = (unsigned)__builtin_amdgcn_ballot_w64(tag_ok(light, tag)) & 0xffu;
    unsigned got = 0;
#pragma unroll
    for (int k = 0; k < 8; ++k) got |= (all_lanes(tag_ok(t[k], tag)) ? 1u : 0u) << k;
    got &= want;
#pragma unroll
    for (int k = 0; k < 8; ++k) {
      const bool gk = (got >> k) & 1u;
      a[k] = gk ? u2f(t[k].x) : a[k];
      b[k] = gk ? u2f(t[k].z) : b[k];
    }
    pending &= ~got;
    if ((pending == 0u && xok) || spins > SPIN_CAP) break;
    want = __builtin_popcount(pending) <= 2 ? pending : (pending & seen);
    __builtin_amdgcn_s_sleep(1);
  }
  s0 = 0.f; s1 = 0.f;
#pragma unroll
  for (int k = 0; k < 8; ++k) { s0 += a[k]; s1 += b[k]; }
}

struct Ctx {
  LAS unsigned char* L;
  int lane, g, c, b, h, s, wave;
  unsigned t_start;
  v4u mask;
};

__device__ __forceinline__ void stream_role(const Params& p, const Ctx& x) {
  LAS unsigned char* L = x.L;
  const int lane = x.lane, c = x.c, g = x.g, h = x.h, s = x.s, w = x.wave;
  const v4u mask = x.mask;
  const bool has_ffn = h < NFG;
  const size_t slice = ((size_t)h * SK + (size_t)s * KPS) * DH * 2 + w * 1024;
  const __amdgpu_buffer_rsrc_t rsK = make_rsrc((const char*)p.k + slice, KPS * DH * 2 - w * 1024);
  const __amdgpu_buffer_rsrc_t rsV = make_rsrc((const char*)p.v + slice, KPS * DH * 2 - w * 1024);
  const __amdgpu_buffer_rsrc_t rsO = make_rsrc((const char*)p.wo + ((size_t)(h * DH + 32 * w) * D + 80 * s) * 2, 32 * D * 2 - 160 * s);
  const __amdgpu_buffer_rsrc_t rs2 =
      make_rsrc((const char*)p.w2 + (has_ffn ? ((size_t)(h * 64 + 16 * w) * D + 80 * s) * 2 : 0), has_ffn ? 16 * D * 2 - 160 * s : 0);
  const unsigned voff = lane * 16, vo = c < 10 ? g * (D * 2) + c * 16 : OOR;
  auto ld = [&](auto jc) -> v4u {
    constexpr int j = decltype(jc)::value;
    if constexpr (j < 64) return bload<AUX_NT>(rsK, voff, j * 4096);
    else return bload<AUX_NT>(rsV, voff, (j - 64) * 4096);
  };
  auto ldw = [&](auto tc) -> v4u {
    constexpr int t = decltype(tc)::value;
    if constexpr (t < 8) return bload<AUX_DEF>(rsO, vo, t * 4 * D * 2);
    else return bload<AUX_DEF>(rs2, vo, (t - 8) * 4 * D * 2);
  };
  v4u ring[RING], wt[12];
  unsigned tz = x.t_start;

  asm volatile("s_barrier" ::: "memory");
  static_for<0, RING>([&](auto jc) {
    constexpr int j = decltype(jc)::value;
    if constexpr (j >= INFL) asm volatile("s_waitcnt vmcnt(%0)" ::"i"(INFL - 1) : "memory");
    if constexpr (j >= 5) wait_until(tz + due_at(j));
    ring[j] = ld(jc);
    __builtin_amdgcn_sched_barrier(0);
  });
  WG_BARRIER();
  const v4u q = *(const LAS v4u*)(L + L_Q + c * 16);
  const v4u tg = *(const LAS v4u*)(L + L_TAG);
  const v2u tag = {tg.x, tg.y};
  tz = (unsigned)__builtin_amdgcn_readfirstlane((int)tg.z);

  float st0 = 0.f, st1 = 0.f, st2 = 0.f, st3 = 0.f;
  float sc[4];
  unsigned pd[4];
  f32x4 acc = {0.f, 0.f, 0.f, 0.f};
  auto process = [&](auto tc) {
    constexpr int T = decltype(tc)::value;
    const v4u d = ring[T % RING];
    if constexpr (T < 64) {
      constexpr int t = T & 15;
      float v = dot2(d.x, q.x, 0.f); v = dot2(d.y, q.y, v); v = dot2(d.z, q.z, v); v = dot2(d.w, q.w, v);
      if constexpr ((t & 1) == 0) st0 = v;
      else {
        v = tr_merge<8, ROR8>(st0, v, c);
        if constexpr ((t & 2) == 0) st1 = v;
        else {
          v = tr_merge<4, XOR7>(st1, v, c);
          if constexpr ((t & 4) == 0) st2 = v;
          else {
            v = tr_merge<2, XOR2>(st2, v, c);
            if constexpr ((t & 8) == 0) st3 = v;
            else sc[T >> 4] = tr_merge<1, XOR1>(st3, v, c);
          }
        }
      }
      if constexpr (T == 63) {
        float sv[4];
#pragma unroll
        for (int i = 0; i < 4; ++i) sv[i] = rbf(rbf(sc[i]) * 0.08838834764831845f);
        const float mw = wave_max(fmaxf(fmaxf(sv[0], sv[1]), fmaxf(sv[2], sv[3])));
        float e = 0.f;
#pragma unroll
        for (int i = 0; i < 4; ++i) { const float pe = __builtin_amdgcn_exp2f((sv[i] - mw) * LOG2E); e += pe; pd[i] = dup_bf16(pe); }
        const float lw = wave_sum(e);
        if (lane == 0) {
          *(LAS f32x2*)(L + L_ST + w * 8) = f32x2{mw, lw};
          __hip_atomic_fetch_add((LAS unsigned*)(L + L_CNT), 1u, __ATOMIC_RELAXED, __HIP_MEMORY_SCOPE_WORKGROUP);
        }
      }
    } else {
      constexpr int t = (T - 64) & 15, G = (T - 64) >> 4;
      acc = sel_mfma(dppu<BCAST + bitrev4(t)>(pd[G]), mask, d, acc);
    }
  };
  static_for<RING, NT_KV>([&](auto jc) {
    constexpr int j = decltype(jc)::value;
    asm volatile("s_waitcnt vmcnt(%0)" ::"i"(INFL - 1) : "memory");
    wait_until(tz + due_at(j));
    static_for<done_after(j - 1), done_after(j)>([&](auto tc) { process(tc); });
    ring[j % RING] = ld(jc);
    __builtin_amdgcn_sched_barrier(0);
  });
  static_for<done_after(NT_KV - 1), NT_KV>([&](auto tc) {
    constexpr int i = decltype(tc)::value - done_after(NT_KV - 1), n = NT_KV - done_after(NT_KV - 1);
    process(tc);
    static_for<12 * i / n, 12 * (i + 1) / n>([&](auto wc) {
      constexpr int k = (decltype(wc)::value + 8) % 12;
      wt[k] = ldw(std::integral_constant<int, k>{});
    });
    __builtin_amdgcn_sched_barrier(0);
  });
  if (lane < 32) *(LAS f32x4*)(L + L_REC + w * 512 + (8 * c + 4 * g) * 4) = acc;
  WG_BARRIER();

  const __amdgpu_buffer_rsrc_t rsW = make_rsrc(p.ws, WS_END);
  unsigned cf[4];
#pragma unroll
  for (int t = 0; t < 4; ++t) cf[t] = *(const LAS unsigned*)(L + L_HID + (16 * w + 4 * t + g) * 4);
  __builtin_amdgcn_s_sleep(O_SLEEP);
  v4u ga, gb;
  {
    const unsigned go = g * O_STRIDE + c * 16, so = WS_O + h * NSPLIT * O_STRIDE + 256 * w;
    auto fetch = [&]() {
      COMPILER_FENCE();
      ga = bload<AUX_SC1>(rsW, go, so);
      gb = bload<AUX_SC1>(rsW, go, so + 4 * O_STRIDE);
    };
    auto arrived = [&]() { return all_lanes(tag_ok(ga, tag) && tag_ok(gb, tag)); };
    fetch();
    if (__builtin_expect(!arrived(), 0)) {
      for (unsigned spins = 0; spins < SPIN_CAP; ++spins) {
        fetch();
        if (arrived()) break;
      }
    }
  }
  {
    const float o0 = xrow_sum(u2f(ga.x) + u2f(gb.x)), o1 = xrow_sum(u2f(ga.z) + u2f(gb.z));
    const int pick = (int)dup_bf16((g & 1) ? o1 : o0);
    const unsigned od = (unsigned)__builtin_amdgcn_update_dpp(pick, pick, ROR8, 0xc, 0xf, false);
    f32x4 acc0 = {0.f, 0.f, 0.f, 0.f}, acc1 = {0.f, 0.f, 0.f, 0.f};
#pragma unroll
    for (int t = 0; t < 4; ++t) acc1 = sel_mfma(cf[t], mask, wt[8 + t], acc1);
    static_for<0, 8>([&](auto tc) {
      constexpr int t = decltype(tc)::value;
      if constexpr (t & 1) acc1 = sel_mfma(dppu<BCAST + t>(od), mask, wt[t], acc1);
      else acc0 = sel_mfma(dppu<BCAST + t>(od), mask, wt[t], acc0);
    });
    acc0 += acc1;
    if (g < 2 && c < 10) *(LAS f32x4*)(L + L_YP + (w * 80 + 8 * c + 4 * g) * 4) = acc0;
  }
  WG_BARRIER();
  if (w == 0) {
    const int l2 = lane < 40 ? lane : 0;
    float y0 = 0.f, y1 = 0.f;
#pragma unroll
    for (int w4 = 0; w4 < 4; ++w4) { const f32x2 t = *(const LAS f32x2*)(L + L_YP + (w4 * 80 + 2 * l2) * 4); y0 += t.x; y1 += t.y; }
    publish(rsW, lane < 40 ? lane * 16 : OOR, WS_Y + h * 5120 + s * 640, y0, y1, tag);
  }
}

__device__ __forceinline__ void side_role(const Params& p, const Ctx& x) {
  LAS unsigned char* L = x.L;
  const int lane = x.lane, c = x.c, g = x.g, h = x.h, s = x.s, b = x.b;
  const v4u mask = x.mask;
  const __amdgpu_buffer_rsrc_t rsW = make_rsrc(p.ws, WS_END);
  const unsigned rec_id = h * NSPLIT + s;
  GAS unsigned long long* epoch = (GAS unsigned long long*)((char*)p.ws + WS_EPOCH);

  __builtin_amdgcn_s_setprio(3);
  unsigned t0;
  const unsigned long long e = __hip_atomic_load(epoch, __ATOMIC_RELAXED, __HIP_MEMORY_SCOPE_AGENT);
  unsigned xr[5];
#pragma unroll
  for (int k = 0; k < 5; ++k) xr[k] = ((const GAS unsigned*)p.x)[lane + 64 * k];
  v4u w[20];
  {
    const __amdgpu_buffer_rsrc_t rs = make_rsrc(p.wq, D * QD * 2);
#pragma unroll
    for (int t = 0; t < 20; ++t) w[t] = bload<AUX_DEF>(rs, g * (QD * 2) + c * 16, ((80 * s + 4 * t) * QD + h * DH) * 2);
  }
  const unsigned xres = ((const GAS unsigned*)p.x)[(b < 160 ? 2 * b : 0) + (lane & 1)];
  __builtin_amdgcn_sched_barrier(0);
  asm volatile("s_barrier" ::: "memory");
  const v2u tag = {(unsigned)e + 1u, (unsigned)(e >> 32) + 1u};
  publish(rsW, (b < 8 && lane == 0) ? WS_T + b * 128 : OOR, 0, u2f(x.t_start), 0.f, tag);
  layernorm_to_lds(xr, L + L_XN, lane);
  LDS_WAIT();
  {
    unsigned cf[20];
#pragma unroll
    for (int t = 0; t < 20; ++t) cf[t] = *(const LAS unsigned*)(L + L_XN + (80 * s + 4 * t + g) * 4);
    f32x4 acc0 = {0.f, 0.f, 0.f, 0.f}, acc1 = {0.f, 0.f, 0.f, 0.f};
#pragma unroll
    for (int t = 0; t < 20; t += 2) { acc0 = sel_mfma(cf[t], mask, w[t], acc0); acc1 = sel_mfma(cf[t + 1], mask, w[t + 1], acc1); }
    acc0 += acc1;
    if (lane < 32) {
      const unsigned vo = (4 * c + 2 * g) * 16;
      publish(rsW, vo, WS_Q + rec_id * 1024, acc0[0], acc0[1], tag);
      publish(rsW, vo + 16, WS_Q + rec_id * 1024, acc0[2], acc0[3], tag);
    }
    __builtin_amdgcn_s_sleep(Q_SLEEP);
    float q0, q1;
    v4u tu;
    gather_sum8<true>(rsW, WS_Q + h * NSPLIT * 1024, tag, lane, q0, q1, lane < 8 ? WS_T + lane * 128 : OOR, tu);
    int dt = lane < 8 ? (int)(tu.x - x.t_start) : 0;
    dt = min(dt, (int)dppu<XOR1>((unsigned)dt)); dt = min(dt, (int)dppu<XOR2>((unsigned)dt)); dt = min(dt, (int)dppu<XOR7>((unsigned)dt));
    dt = max(min(dt, 0), -200);
    t0 = x.t_start + (unsigned)__builtin_amdgcn_readfirstlane(dt);
    *(LAS unsigned*)(L + L_Q + lane * 4) = pack_bf16(q0, q1);
    if (lane == 0) { *(LAS v4u*)(L + L_TAG) = v4u{tag.x, tag.y, t0, 0u}; *(LAS unsigned*)(L + L_CNT) = 0u; }
  }
  WG_BARRIER();
  __builtin_amdgcn_s_setprio(0);
  if (h < NFG) {
    const __amdgpu_buffer_rsrc_t rs1 = make_rsrc(p.w1, D * F * 2), rs3 = make_rsrc(p.w3, D * F * 2);
    const unsigned vo = (4 * (c >> 3) + g) * (F * 2) + (c & 7) * 16;
#pragma unroll
    for (int t = 0; t < 10; ++t) {
      w[2 * t] = bload<AUX_DEF>(rs1, vo, ((80 * s + 8 * t) * F + h * 64) * 2);
      w[2 * t + 1] = bload<AUX_DEF>(rs3, vo, ((80 * s + 8 * t) * F + h * 64) * 2);
    }
    unsigned cf[10];
#pragma unroll
    for (int t = 0; t < 10; ++t) cf[t] = *(const LAS unsigned*)(L + L_XN + (80 * s + 8 * t + 4 * (c >> 3) + g) * 4);
    f32x4 ag = {0.f, 0.f, 0.f, 0.f}, au = {0.f, 0.f, 0.f, 0.f};
#pragma unroll
    for (int t = 0; t < 10; ++t) { ag = sel_mfma(cf[t], mask, w[2 * t], ag); au = sel_mfma(cf[t], mask, w[2 * t + 1], au); }
#pragma unroll
    for (int r = 0; r < 4; ++r) { ag[r] += __shfl(ag[r], (lane + 40) & 63); au[r] += __shfl(au[r], (lane + 40) & 63); }
    if (g < 2 && c < 8) {
      const unsigned uo = (4 * c + 2 * g) * 16;
      publish(rsW, uo, WS_G + rec_id * 1024, ag[0], ag[1], tag);
      publish(rsW, uo + 16, WS_G + rec_id * 1024, ag[2], ag[3], tag);
      publish(rsW, uo + 512, WS_G + rec_id * 1024, au[0], au[1], tag);
      publish(rsW, uo + 528, WS_G + rec_id * 1024, au[2], au[3], tag);
    }
    __builtin_amdgcn_s_sleep(G_SLEEP);
    float a0, a1;
    v4u none;
    gather_sum8<false>(rsW, WS_G + h * NSPLIT * 1024, tag, lane, a0, a1, 0u, none);
    a0 = rbf(a0); a1 = rbf(a1);
    const float u0 = __shfl(a0, (lane + 32) & 63), u1 = __shfl(a1, (lane + 32) & 63);
    const float h0 = rbf(a0 / (1.0f + __expf(-a0))) * u0, h1 = rbf(a1 / (1.0f + __expf(-a1))) * u1;
    if (lane < 32) *(LAS v2u*)(L + L_HID + lane * 8) = v2u{dup_bf16(h0), dup_bf16(h1)};
  } else {
    if (lane < 32) *(LAS v2u*)(L + L_HID + lane * 8) = v2u{0u, 0u};
  }
  float fac[4];
  {
    for (unsigned spins = 0; spins < (SPIN_CAP << 4); ++spins) {
      if (__hip_atomic_load((LAS unsigned*)(L + L_CNT), __ATOMIC_RELAXED, __HIP_MEMORY_SCOPE_WORKGROUP) >= 4u) break;
      __builtin_amdgcn_s_sleep(4);
    }
    COMPILER_FENCE();
    float m[4], l[4];
#pragma unroll
    for (int w4 = 0; w4 < 4; ++w4) { const f32x2 t = *(const LAS f32x2*)(L + L_ST + w4 * 8); m[w4] = t.x; l[w4] = t.y; }
    const float M = fmaxf(fmaxf(m[0], m[1]), fmaxf(m[2], m[3]));
    float ls = 0.f;
#pragma unroll
    for (int w4 = 0; w4 < 4; ++w4) ls = fmaf(__builtin_amdgcn_exp2f((m[w4] - M) * LOG2E), l[w4], ls);
    publish(rsW, lane == 0 ? 1024u : OOR, WS_O + rec_id * O_STRIDE, M, ls, tag);
    __builtin_amdgcn_s_sleep(S_SLEEP);
    v4u ml;
    for (unsigned spins = 0; spins < SPIN_CAP; ++spins) {
      COMPILER_FENCE();
      ml = bload<AUX_SC1>(rsW, lane < 8 ? 1024 + lane * O_STRIDE : OOR, WS_O + h * NSPLIT * O_STRIDE);
      if (all_lanes(lane >= 8 || tag_ok(ml, tag))) break;
      __builtin_amdgcn_s_sleep(16);
    }
    const float mk = u2f(ml.x);
    float MM = fmaxf(mk, dpp<XOR1>(mk)); MM = fmaxf(MM, dpp<XOR2>(MM)); MM = fmaxf(MM, dpp<XOR7>(MM));
    float LL = __builtin_amdgcn_exp2f((mk - MM) * LOG2E) * u2f(ml.z);
    LL += dpp<XOR1>(LL); LL += dpp<XOR2>(LL); LL += dpp<XOR7>(LL);
    const float Mh = u2f((unsigned)__builtin_amdgcn_readfirstlane((int)f2u(MM)));
    const float rl = 1.0f / u2f((unsigned)__builtin_amdgcn_readfirstlane((int)f2u(LL)));
#pragma unroll
    for (int w4 = 0; w4 < 4; ++w4) fac[w4] = __builtin_amdgcn_exp2f((m[w4] - Mh) * LOG2E) * rl;
  }
  const __amdgpu_buffer_rsrc_t rsOut = make_rsrc(p.out, D * 2);
  const unsigned out_off = (b < 160 && lane < 2) ? (2 * b + lane) * 4 : OOR;
  {
    wait_until(t0 + due_at(NT_KV - 1) - PRE_LEAD);
    const v4u p0 = bload<AUX_DEF>(rsW, lane * 16, WS_O + rec_id * O_STRIDE);
    const v4u p1 = bload<AUX_DEF>(rsW, lane < 40 ? lane * 16 : OOR, WS_Y + h * 5120 + s * 640);
    const unsigned p2 = __builtin_amdgcn_raw_buffer_load_b32(rsOut, (int)out_off, 0, AUX_DEF);
    asm volatile("" ::"v"(p0), "v"(p1), "v"(p2));
  }
  __builtin_amdgcn_s_setprio(3);
  WG_BARRIER();
  {
    float o0 = 0.f, o1 = 0.f;
#pragma unroll
    for (int w4 = 0; w4 < 4; ++w4) {
      const f32x2 t = *(const LAS f32x2*)(L + L_REC + w4 * 512 + (32 * g + 4 * (c & 7) + 2 * (c >> 3)) * 4);
      o0 = fmaf(fac[w4], t.x, o0); o1 = fmaf(fac[w4], t.y, o1);
    }
    publish(rsW, lane * 16, WS_O + rec_id * O_STRIDE, o0, o1, tag);
  }
  WG_BARRIER();
  if (b < 160) {
    __builtin_amdgcn_s_sleep(Y_SLEEP);
    const unsigned yo = (lane >> 1) * 5120 + (lane & 1) * 16, ys = WS_Y + b * 32;
    v4u u, ua = bload<AUX_SC1>(rsW, yo, ys);
    for (unsigned spins = 0;; ++spins) {
      __builtin_amdgcn_s_sleep(4);
      COMPILER_FENCE();
      const v4u ub = bload<AUX_SC1>(rsW, yo, ys);
      u = ua;
      if (all_lanes(tag_ok(ua, tag))) break;
      __builtin_amdgcn_s_sleep(4);
      COMPILER_FENCE();
      ua = bload<AUX_SC1>(rsW, yo, ys);
      u = ub;
      if (all_lanes(tag_ok(ub, tag)) || spins > SPIN_CAP) break;
    }
    float y0 = u2f(u.x), y1 = u2f(u.z);
    y0 += dpp<XOR2>(y0); y1 += dpp<XOR2>(y1);
    y0 += dpp<ROR4>(y0); y1 += dpp<ROR4>(y1);
    y0 += dpp<ROR8>(y0); y1 += dpp<ROR8>(y1);
    y0 = xrow_sum(y0); y1 = xrow_sum(y1);
    __builtin_amdgcn_raw_buffer_store_b32(pack_bf16(bflo(xres) + y0, bfhi(xres) + y1), rsOut, (int)out_off, 0, AUX_SC1);
    if (b == 0 && lane == 0) __hip_atomic_store(epoch, ((unsigned long long)tag.y << 32) | tag.x, __ATOMIC_RELAXED, __HIP_MEMORY_SCOPE_AGENT);
  }
}

__global__ void __launch_bounds__(NTHR, 2) fused_decode_kernel(Params p) {
  __shared__ __attribute__((aligned(16))) unsigned char lds_raw[L_END];
  Ctx x;
  x.L = (LAS unsigned char*)lds_raw;
  const int tid = threadIdx.x;
  x.lane = tid & 63; x.wave = __builtin_amdgcn_readfirstlane(tid >> 6);
  x.b = blockIdx.x; x.h = (x.b & 7) * 4 + (x.b >> 6); x.s = (x.b >> 3) & 7;
  x.g = x.lane >> 4; x.c = x.lane & 15;
  {
    const int j = x.c & 7;
    const unsigned half = (j & 1) ? 0xffff0000u : 0x0000ffffu;
    x.mask = v4u{(j >> 1) == 0 ? half : 0u, (j >> 1) == 1 ? half : 0u, (j >> 1) == 2 ? half : 0u, (j >> 1) == 3 ? half : 0u};
  }
  asm volatile("" ::"s"(p.x), "s"(p.k), "s"(p.v), "s"(p.wq), "s"(p.wo), "s"(p.w1), "s"(p.w3), "s"(p.w2), "s"(p.out), "s"(p.ws));
  x.t_start = clock_now();
  if (x.wave < 4) stream_role(p, x);
  else side_role(p, x);
}

}

extern "C" void kernel_launch(void* const* d_in, const int* in_sizes, int n_in, void* d_out, int out_size, void* d_ws, size_t ws_size,
                              hipStream_t stream) {
  Params p;
  p.x = d_in[0]; p.k = d_in[1]; p.v = d_in[2]; p.wq = d_in[3]; p.wo = d_in[4];
  p.w1 = d_in[6]; p.w3 = d_in[7]; p.w2 = d_in[8];
  p.out = d_out; p.ws = d_ws;
  hipLaunchKernelGGL(fused_decode_kernel, dim3(256), dim3(NTHR), 0, stream, p);
}
```
